```python
import torch

def _softmax(x, axis=-1):
    return torch.softmax(x, dim=axis)
def _gelu(x):
    return torch.nn.functional.gelu(x, approximate="tanh")
def _silu(x):
    return torch.nn.functional.silu(x)
def _sigmoid(x):
    return torch.sigmoid(x)
def _layernorm(x, axis=-1, eps=1e-5):
    m = x.mean(dim=axis, keepdim=True)
    v = x.var(dim=axis, keepdim=True, unbiased=False)
    return (x - m) / torch.sqrt(v + eps)
def _scatter_add(tgt, idx, src, axis=0):
    return tgt.clone().index_add_(axis, idx, src)
def _segment_sum(src, seg, n):
    out = torch.zeros((n,) + src.shape[1:], dtype=src.dtype)
    return out.index_add_(0, seg, src)
def _pad(x, pw):
    flat = tuple(v for pair in reversed(pw) for v in pair)
    return torch.nn.functional.pad(x, flat)

kernel_name = 'parallel_sum__mha_block__dense_activation_gelu__prompt'

def setup_inputs():

    _w = (lambda a: (lambda t: t.to(torch.bfloat16) if t.is_floating_point() else t)(torch.as_tensor(a)))
    def _A_setup():
        B, S, D, H = 1, 2048, 512, 32
        return {
            "x": _w((lambda *s: torch.randn(*s, dtype=torch.bfloat16) * 0.05)(B, S, D)),
            "wqkv": _w((lambda *s: torch.randn(*s, dtype=torch.bfloat16) * 0.05)(D, 3 * D)),
            "wo": _w((lambda *s: torch.randn(*s, dtype=torch.bfloat16) * 0.05)(D, D)),
        }
    _A_in = _A_setup()

    def _B_setup():
        B, S, D, F = 1, 2048, 512, 1792
        return {
            "x": _w((lambda *s: torch.randn(*s, dtype=torch.bfloat16) * 0.05)(B, S, D)),
            "w1": _w((lambda *s: torch.randn(*s, dtype=torch.bfloat16) * 0.8)(D, F) * (D ** -0.5)),
            "b1": _w((lambda *s: torch.randn(*s, dtype=torch.bfloat16) * 12.8)(F) * 0.01),
            "w2": _w((lambda *s: torch.randn(*s, dtype=torch.bfloat16) * 0.8)(F, D) * (F ** -0.5)),
            "b2": _w((lambda *s: torch.randn(*s, dtype=torch.bfloat16) * 12.8)(D) * 0.01),
        }
    _B_in = _B_setup()

    return {
        "A_x": _A_in["x"],
        "A_wqkv": _A_in["wqkv"],
        "A_wo": _A_in["wo"],
        "B_x": _B_in["x"],
        "B_w1": _B_in["w1"],
        "B_b1": _B_in["b1"],
        "B_w2": _B_in["w2"],
        "B_b2": _B_in["b2"],
    }

def reference(A_x, A_wqkv, A_wo, B_x, B_w1, B_b1, B_w2, B_b2):
    def _A_ref(x, wqkv, wo):
        B, S, D = x.shape
        H = 32
        Dh = D // H
        qkv = (lambda x, *d: x.permute(*d))((x @ wqkv).reshape(B, S, 3, H, Dh), 2, 0, 3, 1, 4)
        q, k, v = qkv[0], qkv[1], qkv[2]
        scores = torch.einsum("bhqd,bhkd->bhqk", q, k) * (Dh ** -0.5)
        p = _softmax(scores, axis=-1)
        o = torch.einsum("bhqk,bhkd->bhqd", p, v)
        return (lambda x, *d: x.permute(*d))(o, 0, 2, 1, 3).reshape(B, S, D) @ wo

    def _B_ref(x, w1, b1, w2, b2):
        h = torch.einsum("bsd,df->bsf", x, w1) + b1
        h = _gelu(h)
        return torch.einsum("bsf,fd->bsd", h, w2) + b2

    x = A_x
    xn = _layernorm(x, axis=-1)
    return x + _A_ref(xn, A_wqkv, A_wo) + _B_ref(xn, B_w1, B_b1, B_w2, B_b2)
```

```cpp
#include <hip/hip_runtime.h>
#include <hip/hip_bf16.h>
#include <stdint.h>

typedef __hip_bfloat16 bf16;
typedef __attribute__((ext_vector_type(8))) short bf16x8;
typedef __attribute__((ext_vector_type(4))) short s16x4;
typedef __attribute__((ext_vector_type(4))) float f32x4;
typedef __attribute__((ext_vector_type(16))) float f32x16;
typedef __attribute__((ext_vector_type(4))) unsigned u32x4;
typedef __attribute__((ext_vector_type(2))) unsigned u32x2;
typedef __attribute__((address_space(3))) s16x4 lds_s16x4;

constexpr int S_ = 2048, D_ = 512, H_ = 32, F_ = 1792, KA3 = D_ + F_;

#define DI __device__ __forceinline__

DI float bf2f(short u) { return __uint_as_float(((unsigned)(unsigned short)u) << 16); }
DI unsigned pk2(float lo, float hi) {
  __hip_bfloat162 t = __float22bfloat162_rn(make_float2(lo, hi));
  return *reinterpret_cast<unsigned*>(&t);
}
DI float bfround(float v) { return __bfloat162float(__float2bfloat16(v)); }
DI s16x4 tr_read(const char* p) { return __builtin_amdgcn_ds_read_tr16_b64_v4i16((lds_s16x4*)p); }
DI bf16x8 cat8(s16x4 a, s16x4 b) { return __builtin_shufflevector(a, b, 0, 1, 2, 3, 4, 5, 6, 7); }

DI int fswzB512(int k) { return 2 * ((k & 3) | (((k >> 3) & 1) << 2)); }

template <bool VMODE>
DI void k1_loop(f32x4 (&acc)[4][4], const char* As, char* Bs, const bf16* W, int ldw, int col0, int wid, int lane) {
  const int wm = wid >> 2, wn = wid & 3;
  for (int s = 0; s < 16; ++s) {
    __syncthreads();
    if (s + 1 < 16) {
#pragma unroll
      for (int i = 0; i < 2; ++i) {
        const int piece = 2 * wid + i, k = 2 * piece + (lane >> 5), ch = (lane & 31) ^ fswzB512(k);
        const bf16* g = W + (size_t)(32 * (s + 1) + k) * ldw + col0 + ch * 8;
        __builtin_amdgcn_global_load_lds((const unsigned*)g, (unsigned*)(Bs + ((s + 1) & 1) * 16384 + piece * 1024 + lane * 16), 16, 0, 0);
      }
    }
    bf16x8 af[4], bfr[4];
#pragma unroll
    for (int mi = 0; mi < 4; ++mi) {
      const int row = wm * 64 + mi * 16 + (lane & 15);
      af[mi] = *reinterpret_cast<const bf16x8*>(As + row * 1024 + 16 * ((4 * s + (lane >> 4)) ^ (row & 15)));
    }
    const char* Bb = Bs + (s & 1) * 16384;
#pragma unroll
    for (int ni = 0; ni < 4; ++ni) {
      const int g = lane >> 4, q = (lane & 15) >> 2, p = lane & 3, c2 = wn * 8 + ni * 2 + (p >> 1);
      const int k0 = 8 * g + q, k1 = k0 + 4;
      s16x4 lo = tr_read(Bb + 512 * k0 + 16 * (c2 ^ fswzB512(k0)) + 8 * (p & 1));
      s16x4 hi = tr_read(Bb + 512 * k1 + 16 * (c2 ^ fswzB512(k1)) + 8 * (p & 1));
      bfr[ni] = cat8(lo, hi);
    }
#pragma unroll
    for (int mi = 0; mi < 4; ++mi)
#pragma unroll
      for (int ni = 0; ni < 4; ++ni)
        acc[mi][ni] = VMODE ? __builtin_amdgcn_mfma_f32_16x16x32_bf16(af[mi], bfr[ni], acc[mi][ni], 0, 0, 0)
                            : __builtin_amdgcn_mfma_f32_16x16x32_bf16(bfr[ni], af[mi], acc[mi][ni], 0, 0, 0);
  }
}

DI float gelu_tanh(float v) {
  const float u = 0.7978845608028654f * (v + 0.044715f * v * v * v);
  const float e = __builtin_amdgcn_exp2f(u * 2.8853900817779268f);
  const float t = 1.f - 2.f * __builtin_amdgcn_rcpf(e + 1.f);
  return 0.5f * v * (1.f + t);
}

__global__ __launch_bounds__(512, 2) void k1_kernel(const bf16* __restrict__ x, const bf16* __restrict__ wqkv, const bf16* __restrict__ w1,
                                                  const bf16* __restrict__ b1, bf16* __restrict__ Qo, bf16* __restrict__ Ko,
                                                  bf16* __restrict__ Vo, bf16* __restrict__ A3) {
  extern __shared__ __attribute__((aligned(16))) char smem[];
  char* As = smem;
  char* Bs = smem + 131072;
  const int tid = threadIdx.x, wid = tid >> 6, lane = tid & 63;
  const int mt = blockIdx.x & 15, nt = blockIdx.x >> 4;
  const bf16* W;
  int ldw, col0;
  if (nt < 6) { W = wqkv; ldw = 3 * D_; col0 = nt * 256; } else { W = w1; ldw = F_; col0 = (nt - 6) * 256; }
#pragma unroll
  for (int i = 0; i < 2; ++i) {
    const int piece = 2 * wid + i, k = 2 * piece + (lane >> 5), ch = (lane & 31) ^ fswzB512(k);
    const bf16* g = W + (size_t)k * ldw + col0 + ch * 8;
    __builtin_amdgcn_global_load_lds((const unsigned*)g, (unsigned*)(Bs + piece * 1024 + lane * 16), 16, 0, 0);
  }
#pragma unroll
  for (int grp = 0; grp < 2; ++grp) {
    const int row = wid * 16 + grp * 8 + (lane >> 3);
    const bf16* xr = x + (size_t)(mt * 128 + row) * D_;
    bf16x8 xv[8];
#pragma unroll
    for (int i = 0; i < 8; ++i) xv[i] = *reinterpret_cast<const bf16x8*>(xr + ((lane & 7) + 8 * i) * 8);
    float sum = 0.f;
#pragma unroll
    for (int i = 0; i < 8; ++i)
#pragma unroll
      for (int j = 0; j < 8; ++j) sum += bf2f(xv[i][j]);
    sum += __shfl_xor(sum, 1); sum += __shfl_xor(sum, 2); sum += __shfl_xor(sum, 4);
    const float mean = sum * (1.f / D_);
    float sq = 0.f;
#pragma unroll
    for (int i = 0; i < 8; ++i)
#pragma unroll
      for (int j = 0; j < 8; ++j) { const float d = bf2f(xv[i][j]) - mean; sq += d * d; }
    sq += __shfl_xor(sq, 1); sq += __shfl_xor(sq, 2); sq += __shfl_xor(sq, 4);
    const float var = bfround(sq * (1.f / D_));
    const float sd = bfround(__builtin_sqrtf(bfround(var + 1e-5f)));
    const float mb = bfround(mean);
    const float rs = 1.f / sd;
#pragma unroll
    for (int i = 0; i < 8; ++i) {
      u32x4 o;
#pragma unroll
      for (int j = 0; j < 4; ++j) o[j] = pk2((bf2f(xv[i][2 * j]) - mb) * rs, (bf2f(xv[i][2 * j + 1]) - mb) * rs);
      const int ch = (lane & 7) + 8 * i;
      *reinterpret_cast<u32x4*>(As + row * 1024 + 16 * (ch ^ (row & 15))) = o;
    }
  }
  f32x4 acc[4][4] = {};
  const int wm = wid >> 2, wn = wid & 3;
  const bool isV = (nt == 4 || nt == 5);
  if (isV) {
    k1_loop<true>(acc, As, Bs, W, ldw, col0, wid, lane);
#pragma unroll
    for (int mi = 0; mi < 4; ++mi)
#pragma unroll
      for (int ni = 0; ni < 4; ++ni) {
        const int tok = mt * 128 + wm * 64 + mi * 16 + 4 * (lane >> 4);
        const int n = col0 - 1024 + wn * 64 + ni * 16 + (lane & 15);
        const int head = n >> 4, d = n & 15;
        const int g = tok >> 4, t16 = tok & 15, h = (t16 >> 2) & 1, j0 = (t16 >> 3) * 4;
        u32x2 o = {pk2(acc[mi][ni][0], acc[mi][ni][1]), pk2(acc[mi][ni][2], acc[mi][ni][3])};
        *reinterpret_cast<u32x2*>((char*)Vo + ((size_t)(head * 128 + g) * 2 + h) * 256 + d * 16 + j0 * 2) = o;
      }
  } else {
    k1_loop<false>(acc, As, Bs, W, ldw, col0, wid, lane);
    if (nt < 4) {
      const bool isQ = nt < 2;
      const float sc = isQ ? 0.25f * 1.4426950408889634f : 1.f;
#pragma unroll
      for (int mi = 0; mi < 4; ++mi)
#pragma unroll
        for (int ni = 0; ni < 4; ++ni) {
          const int tok = mt * 128 + wm * 64 + mi * 16 + (lane & 15);
          const int n = col0 - (isQ ? 0 : 512) + wn * 64 + ni * 16 + 4 * (lane >> 4);
          const int head = n >> 4, d = n & 15;
          u32x2 o = {pk2(acc[mi][ni][0] * sc, acc[mi][ni][1] * sc), pk2(acc[mi][ni][2] * sc, acc[mi][ni][3] * sc)};
          if (isQ) {
            *reinterpret_cast<u32x2*>(Qo + ((size_t)head * S_ + tok) * 16 + d) = o;
          } else {
            *reinterpret_cast<u32x2*>((char*)Ko + ((size_t)(head * 64 + (tok >> 5)) * 2 + (d >> 3)) * 512 + (tok & 31) * 16 + (d & 7) * 2) = o;
          }
        }
    } else {
#pragma unroll
      for (int ni = 0; ni < 4; ++ni) {
        const int n = col0 + wn * 64 + ni * 16 + 4 * (lane >> 4);
        const s16x4 bb = *reinterpret_cast<const s16x4*>(b1 + n);
#pragma unroll
        for (int mi = 0; mi < 4; ++mi) {
          const int tok = mt * 128 + wm * 64 + mi * 16 + (lane & 15);
          u32x2 o = {pk2(gelu_tanh(acc[mi][ni][0] + bf2f(bb[0])), gelu_tanh(acc[mi][ni][1] + bf2f(bb[1]))),
                     pk2(gelu_tanh(acc[mi][ni][2] + bf2f(bb[2])), gelu_tanh(acc[mi][ni][3] + bf2f(bb[3])))};
          *reinterpret_cast<u32x2*>(A3 + (size_t)tok * KA3 + D_ + n) = o;
        }
      }
    }
  }
}

constexpr float THR_ = 8.f;
constexpr int K2_KOFF = 0, K2_VOFF = 65536, K2_OOFF = 131072, K2_LDS = 131072 + 1024;

DI float max16(const f32x16& s) {
  float m = fmaxf(fmaxf(s[0], s[1]), s[2]);
#pragma unroll
  for (int r = 3; r < 15; r += 2) m = fmaxf(fmaxf(m, s[r]), s[r + 1]);
  return fmaxf(m, s[15]);
}

DI void sm_tile(f32x16& s, f32x16& negm, f32x16& o, bool first, bf16x8& p0, bf16x8& p1) {
  const float mx = max16(s);
  if (first || __any(mx > THR_)) {
    auto rr = __builtin_amdgcn_permlane32_swap(__float_as_uint(mx), __float_as_uint(mx), false, false);
    float d = fmaxf(__uint_as_float(rr[0]), __uint_as_float(rr[1]));
    if (!first) d = fmaxf(d, 0.f);
    const float al = __builtin_amdgcn_exp2f(-d);
#pragma unroll
    for (int r = 0; r < 16; ++r) { s[r] -= d; negm[r] -= d; o[r] *= al; }
  }
  u32x4 a, b;
#pragma unroll
  for (int r = 0; r < 4; ++r) {
    a[r] = pk2(__builtin_amdgcn_exp2f(s[2 * r]), __builtin_amdgcn_exp2f(s[2 * r + 1]));
    b[r] = pk2(__builtin_amdgcn_exp2f(s[8 + 2 * r]), __builtin_amdgcn_exp2f(s[8 + 2 * r + 1]));
  }
  p0 = __builtin_bit_cast(bf16x8, a);
  p1 = __builtin_bit_cast(bf16x8, b);
}

__global__ __launch_bounds__(512, 2) void k2_kernel(const bf16* __restrict__ Qg, const bf16* __restrict__ Kg, const bf16* __restrict__ Vg,
                                                  bf16* __restrict__ A3) {
  extern __shared__ __attribute__((aligned(16))) char smem[];
  const int tid = threadIdx.x, wid = tid >> 6, lane = tid & 63;
  const int head = blockIdx.x >> 3, qb = blockIdx.x & 7;
  {
    const char* kg = (const char*)Kg + (size_t)head * 65536;
    const char* vg = (const char*)Vg + (size_t)head * 65536;
#pragma unroll
    for (int i = 0; i < 8; ++i) {
      const int off = (i * 8 + wid) * 1024 + lane * 16;
      __builtin_amdgcn_global_load_lds((const unsigned*)(kg + off), (unsigned*)(smem + K2_KOFF + off), 16, 0, 0);
      __builtin_amdgcn_global_load_lds((const unsigned*)(vg + off), (unsigned*)(smem + K2_VOFF + off), 16, 0, 0);
    }
    if (tid < 64) { u32x4 ones = {0x3F803F80u, 0x3F803F80u, 0x3F803F80u, 0x3F803F80u}; *reinterpret_cast<u32x4*>(smem + K2_OOFF + tid * 16) = ones; }
  }
  const int qg = wid & 3, kh = wid >> 2, h = lane >> 5, r32 = lane & 31;
  const int q0 = qb * 256 + qg * 64;
  const bf16x8 qA = *reinterpret_cast<const bf16x8*>(Qg + ((size_t)head * S_ + q0 + r32) * 16 + 8 * h);
  const bf16x8 qB = *reinterpret_cast<const bf16x8*>(Qg + ((size_t)head * S_ + q0 + 32 + r32) * 16 + 8 * h);
  f32x16 oA = {}, oB = {}, nmA = {}, nmB = {};
  __syncthreads();
  const char* kp = smem + K2_KOFF + kh * 32768 + lane * 16;
  const char* vp = (r32 < 16) ? smem + K2_VOFF + kh * 32768 + h * 256 + r32 * 16 : smem + K2_OOFF;
  const int vstep = (r32 < 16) ? 1024 : 0;
  for (int t = 0; t < 32; ++t) {
    const bf16x8 kf = *reinterpret_cast<const bf16x8*>(kp + t * 1024);
    const bf16x8 v0 = *reinterpret_cast<const bf16x8*>(vp);
    const bf16x8 v1 = *reinterpret_cast<const bf16x8*>(vp + 512 * (r32 < 16));
    vp += vstep;
    f32x16 sA = __builtin_amdgcn_mfma_f32_32x32x16_bf16(kf, qA, nmA, 0, 0, 0);
    f32x16 sB = __builtin_amdgcn_mfma_f32_32x32x16_bf16(kf, qB, nmB, 0, 0, 0);
    bf16x8 p0, p1;
    sm_tile(sA, nmA, oA, t == 0, p0, p1);
    oA = __builtin_amdgcn_mfma_f32_32x32x16_bf16(v0, p0, oA, 0, 0, 0);
    oA = __builtin_amdgcn_mfma_f32_32x32x16_bf16(v1, p1, oA, 0, 0, 0);
    sm_tile(sB, nmB, oB, t == 0, p0, p1);
    oB = __builtin_amdgcn_mfma_f32_32x32x16_bf16(v0, p0, oB, 0, 0, 0);
    oB = __builtin_amdgcn_mfma_f32_32x32x16_bf16(v1, p1, oB, 0, 0, 0);
  }
  __syncthreads();
  float* ex = reinterpret_cast<float*>(smem) + (qg * 2) * 64 * 10;
  if (kh == 1) {
#pragma unroll
    for (int r = 0; r < 8; ++r) { ex[r * 64 + lane] = oA[r]; ex[640 + r * 64 + lane] = oB[r]; }
    ex[8 * 64 + lane] = oA[8]; ex[9 * 64 + lane] = nmA[0];
    ex[640 + 8 * 64 + lane] = oB[8]; ex[640 + 9 * 64 + lane] = nmB[0];
  }
  __syncthreads();
  if (kh == 0) {
#pragma unroll
    for (int tq = 0; tq < 2; ++tq) {
      const f32x16& o = tq ? oB : oA;
      const float nm0 = tq ? nmB[0] : nmA[0];
      const float* e = ex + tq * 640;
      const float nm1 = e[9 * 64 + lane];
      const float nm = fminf(nm0, nm1);
      const float a0 = __builtin_amdgcn_exp2f(nm - nm0), a1 = __builtin_amdgcn_exp2f(nm - nm1);
      const float l = o[8] * a0 + e[8 * 64 + lane] * a1;
      const float rl = 1.f / l;
      float v[8];
#pragma unroll
      for (int r = 0; r < 8; ++r) v[r] = (o[r] * a0 + e[r * 64 + lane] * a1) * rl;
      const int tok = q0 + tq * 32 + r32;
      bf16* dst = A3 + (size_t)tok * KA3 + head * 16 + 4 * h;
      u32x2 w0 = {pk2(v[0], v[1]), pk2(v[2], v[3])}, w1 = {pk2(v[4], v[5]), pk2(v[6], v[7])};
      *reinterpret_cast<u32x2*>(dst) = w0;
      *reinterpret_cast<u32x2*>(dst + 8) = w1;
    }
  }
}

DI int fswzB128(int k) { return 2 * (((k >> 1) & 1) | (((k >> 3) & 1) << 1)); }
constexpr int K3_NST = 2;

__global__ __launch_bounds__(256, 1) void k3_kernel(const bf16* __restrict__ A3, const bf16* __restrict__ wo, const bf16* __restrict__ w2,
                                                  const bf16* __restrict__ b2, const bf16* __restrict__ x, bf16* __restrict__ out) {
  extern __shared__ __attribute__((aligned(16))) char smem[];
  const int tid = threadIdx.x, wid = tid >> 6, lane = tid & 63;
  const int mt = blockIdx.x >> 3, nt = blockIdx.x & 7;
  const int wm = wid >> 1, wn = wid & 1;
  auto stage = [&](int s, int buf) {
    char* Ab = smem + buf * 16384;
    char* Bb = Ab + 8192;
#pragma unroll
    for (int i = 0; i < 2; ++i) {
      const int piece = 2 * wid + i, row = piece * 8 + (lane >> 3), ch = (lane & 7) ^ (row & 7);
      const bf16* g = A3 + (size_t)(mt * 64 + row) * KA3 + s * 64 + ch * 8;
      __builtin_amdgcn_global_load_lds((const unsigned*)g, (unsigned*)(Ab + piece * 1024 + lane * 16), 16, 0, 0);
    }
#pragma unroll
    for (int i = 0; i < 2; ++i) {
      const int piece = 2 * wid + i, k = piece * 8 + (lane >> 3), ch = (lane & 7) ^ fswzB128(k);
      const int kg = s * 64 + k;
      const bf16* g = (kg < D_ ? wo + (size_t)kg * D_ : w2 + (size_t)(kg - D_) * D_) + nt * 64 + ch * 8;
      __builtin_amdgcn_global_load_lds((const unsigned*)g, (unsigned*)(Bb + piece * 1024 + lane * 16), 16, 0, 0);
    }
  };
  f32x4 acc[2][2] = {};
  constexpr int NS = KA3 / 64;
  stage(0, 0);
  for (int s = 0; s < NS; ++s) {
    __syncthreads();
    if (s + 1 < NS) stage(s + 1, (s + 1) & 1);
    const char* Ab = smem + (s & 1) * 16384;
    const char* Bb = Ab + 8192;
#pragma unroll
    for (int ks = 0; ks < 2; ++ks) {
      bf16x8 af[2], bfr[2];
#pragma unroll
      for (int mi = 0; mi < 2; ++mi) {
        const int row = wm * 32 + mi * 16 + (lane & 15);
        af[mi] = *reinterpret_cast<const bf16x8*>(Ab + row * 128 + 16 * ((4 * ks + (lane >> 4)) ^ (row & 7)));
      }
#pragma unroll
      for (int ni = 0; ni < 2; ++ni) {
        const int g = lane >> 4, q = (lane & 15) >> 2, p = lane & 3, c2 = wn * 4 + ni * 2 + (p >> 1);
        const int k0 = 32 * ks + 8 * g + q, k1 = k0 + 4;
        s16x4 lo = tr_read(Bb + 128 * k0 + 16 * (c2 ^ fswzB128(k0)) + 8 * (p & 1));
        s16x4 hi = tr_read(Bb + 128 * k1 + 16 * (c2 ^ fswzB128(k1)) + 8 * (p & 1));
        bfr[ni] = cat8(lo, hi);
      }
#pragma unroll
      for (int mi = 0; mi < 2; ++mi)
#pragma unroll
        for (int ni = 0; ni < 2; ++ni) acc[mi][ni] = __builtin_amdgcn_mfma_f32_16x16x32_bf16(bfr[ni], af[mi], acc[mi][ni], 0, 0, 0);
    }
  }
#pragma unroll
  for (int ni = 0; ni < 2; ++ni) {
    const int n = nt * 64 + wn * 32 + ni * 16 + 4 * (lane >> 4);
    const s16x4 bb = *reinterpret_cast<const s16x4*>(b2 + n);
#pragma unroll
    for (int mi = 0; mi < 2; ++mi) {
      const int tok = mt * 64 + wm * 32 + mi * 16 + (lane & 15);
      const s16x4 xx = *reinterpret_cast<const s16x4*>(x + (size_t)tok * D_ + n);
      u32x2 o = {pk2(acc[mi][ni][0] + bf2f(bb[0]) + bf2f(xx[0]), acc[mi][ni][1] + bf2f(bb[1]) + bf2f(xx[1])),
                 pk2(acc[mi][ni][2] + bf2f(bb[2]) + bf2f(xx[2]), acc[mi][ni][3] + bf2f(bb[3]) + bf2f(xx[3]))};
      *reinterpret_cast<u32x2*>(out + (size_t)tok * D_ + n) = o;
    }
  }
}

extern "C" void kernel_launch(void* const* d_in, const int* in_sizes, int n_in, void* d_out, int out_size, void* d_ws, size_t ws_size,
                              hipStream_t stream) {
  const bf16* x = (const bf16*)d_in[0];
  const bf16* wqkv = (const bf16*)d_in[1];
  const bf16* wo = (const bf16*)d_in[2];
  const bf16* w1 = (const bf16*)d_in[4];
  const bf16* b1 = (const bf16*)d_in[5];
  const bf16* w2 = (const bf16*)d_in[6];
  const bf16* b2 = (const bf16*)d_in[7];
  bf16* out = (bf16*)d_out;
  char* ws = (char*)d_ws;
  bf16* Q = (bf16*)(ws);
  bf16* K = (bf16*)(ws + (2u << 20));
  bf16* V = (bf16*)(ws + (4u << 20));
  bf16* A3 = (bf16*)(ws + (6u << 20));
  hipFuncSetAttribute((const void*)k1_kernel, hipFuncAttributeMaxDynamicSharedMemorySize, 163840);
  hipFuncSetAttribute((const void*)k2_kernel, hipFuncAttributeMaxDynamicSharedMemorySize, K2_LDS);
  hipFuncSetAttribute((const void*)k3_kernel, hipFuncAttributeMaxDynamicSharedMemorySize, K3_NST * 16384);
  k1_kernel<<<208, 512, 163840, stream>>>(x, wqkv, w1, b1, Q, K, V, A3);
  k2_kernel<<<256, 512, K2_LDS, stream>>>(Q, K, V, A3);
  k3_kernel<<<256, 256, K3_NST * 16384, stream>>>(A3, wo, w2, b2, x, out);
}
```

```cpp
#include <hip/hip_runtime.h>
#include <hip/hip_bf16.h>
#include <stdint.h>

typedef __hip_bfloat16 bf16;
typedef __attribute__((ext_vector_type(8))) short bf16x8;
typedef __attribute__((ext_vector_type(4))) short s16x4;
typedef __attribute__((ext_vector_type(4))) float f32x4;
typedef __attribute__((ext_vector_type(16))) float f32x16;
typedef __attribute__((ext_vector_type(4))) unsigned u32x4;
typedef __attribute__((ext_vector_type(2))) unsigned u32x2;
typedef __attribute__((address_space(3))) s16x4 lds_s16x4;

constexpr int S_ = 2048, D_ = 512, H_ = 32, F_ = 1792, KA3 = D_ + F_;

#define DI __device__ __forceinline__

DI float bf2f(short u) { return __uint_as_float(((unsigned)(unsigned short)u) << 16); }
typedef __attribute__((ext_vector_type(2))) float f32x2;
typedef __attribute__((ext_vector_type(2))) __bf16 bf16x2_t;
DI unsigned pk2(float lo, float hi) {
  f32x2 v = {lo, hi};
  return __builtin_bit_cast(unsigned, __builtin_convertvector(v, bf16x2_t));
}
DI float max3(float a, float b, float c) { return fmaxf(fmaxf(a, b), c); }
DI void glds16(const void* g, const char* lds_lane0) {
  const unsigned dst = __builtin_amdgcn_readfirstlane((unsigned)(size_t)lds_lane0);
  unsigned keep;
  asm volatile("s_mov_b32 %0, m0\n\ts_mov_b32 m0, %2\n\ts_nop 0\n\tglobal_load_lds_dwordx4 %1, off\n\ts_mov_b32 m0, %0"
               : "=&s"(keep) : "v"(g), "s"(dst) : "memory");
}
DI float bfround(float v) { return __bfloat162float(__float2bfloat16(v)); }
DI s16x4 tr_read(const char* p) { return __builtin_amdgcn_ds_read_tr16_b64_v4i16((lds_s16x4*)p); }
DI bf16x8 cat8(s16x4 a, s16x4 b) { return __builtin_shufflevector(a, b, 0, 1, 2, 3, 4, 5, 6, 7); }

#define WAITV(n) asm volatile("s_waitcnt vmcnt(" #n ")" ::: "memory")
#define BAR() __builtin_amdgcn_s_barrier()

DI void k1_stage(char* Bs, const bf16* W, int ldw, int col0, int s, int wid, int lane) {
  const int k = 2 * wid + (lane >> 5), ch = (lane & 31) ^ (4 * (k & 3));
  const bf16* g = W + (size_t)(16 * s + k) * ldw + col0 + ch * 8;
  glds16(g, Bs + (s & 3) * 8192 + wid * 1024);
}

template <bool VMODE>
DI void k1_loop(f32x16 (&acc)[2][2], const char* As, char* Bs, const bf16* W, int ldw, int col0, int wid, int lane) {
  const int wm = wid >> 2, wn = wid & 3, h = lane >> 5;
  for (int s = 0; s < 32; ++s) {
    if (s < 29) WAITV(2); else WAITV(0);
    BAR();
    if (s < 29) k1_stage(Bs, W, ldw, col0, s + 3, wid, lane);
    bf16x8 af[2], bfr[2];
#pragma unroll
    for (int mi = 0; mi < 2; ++mi) {
      const int row = wm * 64 + mi * 32 + (lane & 31);
      af[mi] = *reinterpret_cast<const bf16x8*>(As + row * 1024 + 16 * ((2 * s + h) ^ (row & 15)));
    }
    const char* Bb = Bs + (s & 3) * 8192;
#pragma unroll
    for (int ni = 0; ni < 2; ++ni) {
      const int q = (lane & 15) >> 2, p = lane & 3, c2 = wn * 8 + ni * 4 + 2 * ((lane >> 4) & 1) + (p >> 1);
      const int k0 = 8 * h + q, k1 = k0 + 4;
      s16x4 lo = tr_read(Bb + 512 * k0 + 16 * (c2 ^ (4 * (k0 & 3))) + 8 * (p & 1));
      s16x4 hi = tr_read(Bb + 512 * k1 + 16 * (c2 ^ (4 * (k1 & 3))) + 8 * (p & 1));
      bfr[ni] = cat8(lo, hi);
    }
#pragma unroll
    for (int mi = 0; mi < 2; ++mi)
#pragma unroll
      for (int ni = 0; ni < 2; ++ni)
        acc[mi][ni] = VMODE ? __builtin_amdgcn_mfma_f32_32x32x16_bf16(af[mi], bfr[ni], acc[mi][ni], 0, 0, 0)
                            : __builtin_amdgcn_mfma_f32_32x32x16_bf16(bfr[ni], af[mi], acc[mi][ni], 0, 0, 0);
  }
}

DI float gelu_tanh(float v) {
  const float u = 0.7978845608028654f * (v + 0.044715f * v * v * v);
  const float e = __builtin_amdgcn_exp2f(u * 2.8853900817779268f);
  const float t = 1.f - 2.f * __builtin_amdgcn_rcpf(e + 1.f);
  return 0.5f * v * (1.f + t);
}

__global__ __launch_bounds__(512, 2) void k1_kernel(const bf16* __restrict__ x, const bf16* __restrict__ wqkv, const bf16* __restrict__ w1,
                                                  const bf16* __restrict__ b1, bf16* __restrict__ Qo, bf16* __restrict__ Ko,
                                                  bf16* __restrict__ Vo, bf16* __restrict__ A3) {
  extern __shared__ __attribute__((aligned(16))) char smem[];
  char* As = smem;
  char* Bs = smem + 131072;
  const int tid = threadIdx.x, wid = tid >> 6, lane = tid & 63;
  const int mt = blockIdx.x & 15, nt = blockIdx.x >> 4;
  const bf16* W;
  int ldw, col0;
  if (nt < 6) { W = wqkv; ldw = 3 * D_; col0 = nt * 256; } else { W = w1; ldw = F_; col0 = (nt - 6) * 256; }
  k1_stage(Bs, W, ldw, col0, 0, wid, lane);
  k1_stage(Bs, W, ldw, col0, 1, wid, lane);
  k1_stage(Bs, W, ldw, col0, 2, wid, lane);
#pragma unroll
  for (int grp = 0; grp < 2; ++grp) {
    const int row = wid * 16 + grp * 8 + (lane >> 3);
    const bf16* xr = x + (size_t)(mt * 128 + row) * D_;
    bf16x8 xv[8];
#pragma unroll
    for (int i = 0; i < 8; ++i) xv[i] = *reinterpret_cast<const bf16x8*>(xr + ((lane & 7) + 8 * i) * 8);
    float sum = 0.f;
#pragma unroll
    for (int i = 0; i < 8; ++i)
#pragma unroll
      for (int j = 0; j < 8; ++j) sum += bf2f(xv[i][j]);
    sum += __shfl_xor(sum, 1); sum += __shfl_xor(sum, 2); sum += __shfl_xor(sum, 4);
    const float mean = sum * (1.f / D_);
    float sq = 0.f;
#pragma unroll
    for (int i = 0; i < 8; ++i)
#pragma unroll
      for (int j = 0; j < 8; ++j) { const float d = bf2f(xv[i][j]) - mean; sq += d * d; }
    sq += __shfl_xor(sq, 1); sq += __shfl_xor(sq, 2); sq += __shfl_xor(sq, 4);
    const float var = bfround(sq * (1.f / D_));
    const float sd = bfround(__builtin_sqrtf(bfround(var + 1e-5f)));
    const float mb = bfround(mean);
    const float rs = 1.f / sd;
#pragma unroll
    for (int i = 0; i < 8; ++i) {
      u32x4 o;
#pragma unroll
      for (int j = 0; j < 4; ++j) o[j] = pk2((bf2f(xv[i][2 * j]) - mb) * rs, (bf2f(xv[i][2 * j + 1]) - mb) * rs);
      const int ch = (lane & 7) + 8 * i;
      *reinterpret_cast<u32x4*>(As + row * 1024 + 16 * (ch ^ (row & 15))) = o;
    }
  }
  asm volatile("s_waitcnt lgkmcnt(0)" ::: "memory");
  f32x16 acc[2][2] = {};
  const int wm = wid >> 2, wn = wid & 3, h = lane >> 5, r32 = lane & 31;
  const bool isV = (nt == 4 || nt == 5);
  if (isV) {
    k1_loop<true>(acc, As, Bs, W, ldw, col0, wid, lane);
#pragma unroll
    for (int mi = 0; mi < 2; ++mi)
#pragma unroll
      for (int ni = 0; ni < 2; ++ni)
#pragma unroll
        for (int g4 = 0; g4 < 4; ++g4) {
          const int tok = mt * 128 + wm * 64 + mi * 32 + 8 * g4 + 4 * h;
          const int n = col0 - 1024 + wn * 64 + ni * 32 + r32;
          const int head = n >> 4, d = n & 15;
          const int g = tok >> 4, t16 = tok & 15, hh = (t16 >> 2) & 1, j0 = (t16 >> 3) * 4;
          u32x2 o = {pk2(acc[mi][ni][4 * g4], acc[mi][ni][4 * g4 + 1]), pk2(acc[mi][ni][4 * g4 + 2], acc[mi][ni][4 * g4 + 3])};
          *reinterpret_cast<u32x2*>((char*)Vo + ((size_t)(head * 128 + g) * 2 + hh) * 256 + d * 16 + j0 * 2) = o;
        }
  } else {
    k1_loop<false>(acc, As, Bs, W, ldw, col0, wid, lane);
    if (nt < 4) {
      const bool isQ = nt < 2;
      const float sc = isQ ? 0.25f * 1.4426950408889634f : 1.f;
#pragma unroll
      for (int mi = 0; mi < 2; ++mi)
#pragma unroll
        for (int ni = 0; ni < 2; ++ni)
#pragma unroll
          for (int g4 = 0; g4 < 4; ++g4) {
            const int tok = mt * 128 + wm * 64 + mi * 32 + r32;
            const int n = col0 - (isQ ? 0 : 512) + wn * 64 + ni * 32 + 8 * g4 + 4 * h;
            const int head = n >> 4, d = n & 15;
            u32x2 o = {pk2(acc[mi][ni][4 * g4] * sc, acc[mi][ni][4 * g4 + 1] * sc),
                       pk2(acc[mi][ni][4 * g4 + 2] * sc, acc[mi][ni][4 * g4 + 3] * sc)};
            if (isQ) {
              *reinterpret_cast<u32x2*>(Qo + ((size_t)head * S_ + tok) * 16 + d) = o;
            } else {
              *reinterpret_cast<u32x2*>((char*)Ko + ((size_t)(head * 64 + (tok >> 5)) * 2 + (d >> 3)) * 512 + (tok & 31) * 16 + (d & 7) * 2) = o;
            }
          }
    } else {
#pragma unroll
      for (int ni = 0; ni < 2; ++ni)
#pragma unroll
        for (int g4 = 0; g4 < 4; ++g4) {
          const int n = col0 + wn * 64 + ni * 32 + 8 * g4 + 4 * h;
          const s16x4 bb = *reinterpret_cast<const s16x4*>(b1 + n);
#pragma unroll
          for (int mi = 0; mi < 2; ++mi) {
            const int tok = mt * 128 + wm * 64 + mi * 32 + r32;
            u32x2 o = {pk2(gelu_tanh(acc[mi][ni][4 * g4] + bf2f(bb[0])), gelu_tanh(acc[mi][ni][4 * g4 + 1] + bf2f(bb[1]))),
                       pk2(gelu_tanh(acc[mi][ni][4 * g4 + 2] + bf2f(bb[2])), gelu_tanh(acc[mi][ni][4 * g4 + 3] + bf2f(bb[3])))};
            *reinterpret_cast<u32x2*>(A3 + (size_t)tok * KA3 + D_ + n) = o;
          }
        }
    }
  }
}

constexpr float THR_ = 8.f;
constexpr int K2_KOFF = 0, K2_VOFF = 65536, K2_OOFF = 131072, K2_LDS = 131072 + 1024;

DI float max16(const f32x16& s) {
  float m = max3(s[0], s[1], s[2]);
#pragma unroll
  for (int r = 3; r < 15; r += 2) m = max3(m, s[r], s[r + 1]);
  return max3(m, s[15], s[15]);
}

DI void sm_tile(f32x16& s, f32x16& negm, f32x16& o, bool first, bf16x8& p0, bf16x8& p1) {
  const float mx = max16(s);
  if (first || __any(mx > THR_)) {
    auto rr = __builtin_amdgcn_permlane32_swap(__float_as_uint(mx), __float_as_uint(mx), false, false);
    float d = fmaxf(__uint_as_float(rr[0]), __uint_as_float(rr[1]));
    if (!first) d = fmaxf(d, 0.f);
    const float al = __builtin_amdgcn_exp2f(-d);
#pragma unroll
    for (int r = 0; r < 16; ++r) { s[r] -= d; negm[r] -= d; o[r] *= al; }
  }
  u32x4 a, b;
#pragma unroll
  for (int r = 0; r < 4; ++r) {
    a[r] = pk2(__builtin_amdgcn_exp2f(s[2 * r]), __builtin_amdgcn_exp2f(s[2 * r + 1]));
    b[r] = pk2(__builtin_amdgcn_exp2f(s[8 + 2 * r]), __builtin_amdgcn_exp2f(s[8 + 2 * r + 1]));
  }
  p0 = __builtin_bit_cast(bf16x8, a);
  p1 = __builtin_bit_cast(bf16x8, b);
}

__global__ __launch_bounds__(1024, 4) void k2_kernel(const bf16* __restrict__ Qg, const bf16* __restrict__ Kg, const bf16* __restrict__ Vg,
                                                   bf16* __restrict__ A3) {
  extern __shared__ __attribute__((aligned(16))) char smem[];
  const int tid = threadIdx.x, wid = tid >> 6, lane = tid & 63;
  const int head = (blockIdx.x & 7) + 8 * ((blockIdx.x >> 3) & 3), qb = blockIdx.x >> 5;
  {
    const char* kg = (const char*)Kg + (size_t)head * 65536;
    const char* vg = (const char*)Vg + (size_t)head * 65536;
#pragma unroll
    for (int i = 0; i < 4; ++i) {
      const int off = (i * 16 + wid) * 1024 + lane * 16;
      glds16(kg + off, smem + K2_KOFF + off - lane * 16);
      glds16(vg + off, smem + K2_VOFF + off - lane * 16);
    }
    if (tid < 64) { u32x4 ones = {0x3F803F80u, 0x3F803F80u, 0x3F803F80u, 0x3F803F80u}; *reinterpret_cast<u32x4*>(smem + K2_OOFF + tid * 16) = ones; }
  }
  const int qg = wid & 3, kh = wid >> 2, h = lane >> 5, r32 = lane & 31;
  const int q0 = qb * 256 + qg * 64;
  const bf16x8 qA = *reinterpret_cast<const bf16x8*>(Qg + ((size_t)head * S_ + q0 + r32) * 16 + 8 * h);
  const bf16x8 qB = *reinterpret_cast<const bf16x8*>(Qg + ((size_t)head * S_ + q0 + 32 + r32) * 16 + 8 * h);
  f32x16 oA = {}, oB = {}, nmA = {}, nmB = {};
  WAITV(0);
  __syncthreads();
  const char* kp = smem + K2_KOFF + kh * 16384 + lane * 16;
  const char* vp = (r32 < 16) ? smem + K2_VOFF + kh * 16384 + h * 256 + r32 * 16 : smem + K2_OOFF;
  const int vstep = (r32 < 16) ? 1024 : 0;
  for (int t = 0; t < 16; ++t) {
    const bf16x8 kf = *reinterpret_cast<const bf16x8*>(kp + t * 1024);
    const bf16x8 v0 = *reinterpret_cast<const bf16x8*>(vp);
    const bf16x8 v1 = *reinterpret_cast<const bf16x8*>(vp + 512 * (r32 < 16));
    vp += vstep;
    f32x16 sA = __builtin_amdgcn_mfma_f32_32x32x16_bf16(kf, qA, nmA, 0, 0, 0);
    f32x16 sB = __builtin_amdgcn_mfma_f32_32x32x16_bf16(kf, qB, nmB, 0, 0, 0);
    bf16x8 p0, p1;
    sm_tile(sA, nmA, oA, t == 0, p0, p1);
    oA = __builtin_amdgcn_mfma_f32_32x32x16_bf16(v0, p0, oA, 0, 0, 0);
    oA = __builtin_amdgcn_mfma_f32_32x32x16_bf16(v1, p1, oA, 0, 0, 0);
    sm_tile(sB, nmB, oB, t == 0, p0, p1);
    oB = __builtin_amdgcn_mfma_f32_32x32x16_bf16(v0, p0, oB, 0, 0, 0);
    oB = __builtin_amdgcn_mfma_f32_32x32x16_bf16(v1, p1, oB, 0, 0, 0);
  }
  __syncthreads();
  float* ex = reinterpret_cast<float*>(smem) + qg * (3 * 2 * 640);
  if (kh != 0) {
    float* e = ex + (kh - 1) * 1280;
#pragma unroll
    for (int r = 0; r < 8; ++r) { e[r * 64 + lane] = oA[r]; e[640 + r * 64 + lane] = oB[r]; }
    e[8 * 64 + lane] = oA[8]; e[9 * 64 + lane] = nmA[0];
    e[640 + 8 * 64 + lane] = oB[8]; e[640 + 9 * 64 + lane] = nmB[0];
  }
  __syncthreads();
  if (kh == 0) {
#pragma unroll
    for (int tq = 0; tq < 2; ++tq) {
      const f32x16& o = tq ? oB : oA;
      const float nm0 = tq ? nmB[0] : nmA[0];
      float nm = nm0;
#pragma unroll
      for (int j = 0; j < 3; ++j) nm = fminf(nm, ex[j * 1280 + tq * 640 + 9 * 64 + lane]);
      const float a0 = __builtin_amdgcn_exp2f(nm - nm0);
      float l = o[8] * a0, v[8];
#pragma unroll
      for (int r = 0; r < 8; ++r) v[r] = o[r] * a0;
#pragma unroll
      for (int j = 0; j < 3; ++j) {
        const float* e = ex + j * 1280 + tq * 640;
        const float aj = __builtin_amdgcn_exp2f(nm - e[9 * 64 + lane]);
        l += e[8 * 64 + lane] * aj;
#pragma unroll
        for (int r = 0; r < 8; ++r) v[r] += e[r * 64 + lane] * aj;
      }
      const float rl = 1.f / l;
      const int tok = q0 + tq * 32 + r32;
      bf16* dst = A3 + (size_t)tok * KA3 + head * 16 + 4 * h;
      u32x2 w0 = {pk2(v[0] * rl, v[1] * rl), pk2(v[2] * rl, v[3] * rl)}, w1 = {pk2(v[4] * rl, v[5] * rl), pk2(v[6] * rl, v[7] * rl)};
      *reinterpret_cast<u32x2*>(dst) = w0;
      *reinterpret_cast<u32x2*>(dst + 8) = w1;
    }
  }
}

DI int fswzB128(int k) { return 2 * (((k >> 1) & 1) | (((k >> 3) & 1) << 1)); }
constexpr int K3_NST = 4;

__global__ __launch_bounds__(256, 1) void k3_kernel(const bf16* __restrict__ A3, const bf16* __restrict__ wo, const bf16* __restrict__ w2,
                                                  const bf16* __restrict__ b2, const bf16* __restrict__ x, bf16* __restrict__ out) {
  extern __shared__ __attribute__((aligned(16))) char smem[];
  const int tid = threadIdx.x, wid = tid >> 6, lane = tid & 63;
  const int mt = (blockIdx.x & 7) + 8 * ((blockIdx.x >> 3) & 3), nt = blockIdx.x >> 5;
  const int wm = wid >> 1, wn = wid & 1;
  auto stage = [&](int s) {
    char* Ab = smem + (s & 3) * 16384;
    char* Bb = Ab + 8192;
#pragma unroll
    for (int i = 0; i < 2; ++i) {
      const int piece = 2 * wid + i, row = piece * 8 + (lane >> 3), ch = (lane & 7) ^ (row & 7);
      const bf16* g = A3 + (size_t)(mt * 64 + row) * KA3 + s * 64 + ch * 8;
      glds16(g, Ab + piece * 1024);
    }
#pragma unroll
    for (int i = 0; i < 2; ++i) {
      const int piece = 2 * wid + i, k = piece * 8 + (lane >> 3), ch = (lane & 7) ^ fswzB128(k);
      const int kg = s * 64 + k;
      const bf16* g = (kg < D_ ? wo + (size_t)kg * D_ : w2 + (size_t)(kg - D_) * D_) + nt * 64 + ch * 8;
      glds16(g, Bb + piece * 1024);
    }
  };
  f32x4 acc[2][2] = {};
  constexpr int NS = KA3 / 64;
  stage(0); stage(1); stage(2);
  for (int s = 0; s < NS; ++s) {
    if (s < NS - 3) WAITV(8); else WAITV(0);
    BAR();
    if (s < NS - 3) stage(s + 3);
    const char* Ab = smem + (s & 3) * 16384;
    const char* Bb = Ab + 8192;
#pragma unroll
    for (int ks = 0; ks < 2; ++ks) {
      bf16x8 af[2], bfr[2];
#pragma unroll
      for (int mi = 0; mi < 2; ++mi) {
        const int row = wm * 32 + mi * 16 + (lane & 15);
        af[mi] = *reinterpret_cast<const bf16x8*>(Ab + row * 128 + 16 * ((4 * ks + (lane >> 4)) ^ (row & 7)));
      }
#pragma unroll
      for (int ni = 0; ni < 2; ++ni) {
        const int g = lane >> 4, q = (lane & 15) >> 2, p = lane & 3, c2 = wn * 4 + ni * 2 + (p >> 1);
        const int k0 = 32 * ks + 8 * g + q, k1 = k0 + 4;
        s16x4 lo = tr_read(Bb + 128 * k0 + 16 * (c2 ^ fswzB128(k0)) + 8 * (p & 1));
        s16x4 hi = tr_read(Bb + 128 * k1 + 16 * (c2 ^ fswzB128(k1)) + 8 * (p & 1));
        bfr[ni] = cat8(lo, hi);
      }
#pragma unroll
      for (int mi = 0; mi < 2; ++mi)
#pragma unroll
        for (int ni = 0; ni < 2; ++ni) acc[mi][ni] = __builtin_amdgcn_mfma_f32_16x16x32_bf16(bfr[ni], af[mi], acc[mi][ni], 0, 0, 0);
    }
  }
#pragma unroll
  for (int ni = 0; ni < 2; ++ni) {
    const int n = nt * 64 + wn * 32 + ni * 16 + 4 * (lane >> 4);
    const s16x4 bb = *reinterpret_cast<const s16x4*>(b2 + n);
#pragma unroll
    for (int mi = 0; mi < 2; ++mi) {
      const int tok = mt * 64 + wm * 32 + mi * 16 + (lane & 15);
      const s16x4 xx = *reinterpret_cast<const s16x4*>(x + (size_t)tok * D_ + n);
      u32x2 o = {pk2(acc[mi][ni][0] + bf2f(bb[0]) + bf2f(xx[0]), acc[mi][ni][1] + bf2f(bb[1]) + bf2f(xx[1])),
                 pk2(acc[mi][ni][2] + bf2f(bb[2]) + bf2f(xx[2]), acc[mi][ni][3] + bf2f(bb[3]) + bf2f(xx[3]))};
      *reinterpret_cast<u32x2*>(out + (size_t)tok * D_ + n) = o;
    }
  }
}

extern "C" void kernel_launch(void* const* d_in, const int* in_sizes, int n_in, void* d_out, int out_size, void* d_ws, size_t ws_size,
                              hipStream_t stream) {
  const bf16* x = (const bf16*)d_in[0];
  const bf16* wqkv = (const bf16*)d_in[1];
  const bf16* wo = (const bf16*)d_in[2];
  const bf16* w1 = (const bf16*)d_in[4];
  const bf16* b1 = (const bf16*)d_in[5];
  const bf16* w2 = (const bf16*)d_in[6];
  const bf16* b2 = (const bf16*)d_in[7];
  bf16* out = (bf16*)d_out;
  char* ws = (char*)d_ws;
  bf16* Q = (bf16*)(ws);
  bf16* K = (bf16*)(ws + (2u << 20));
  bf16* V = (bf16*)(ws + (4u << 20));
  bf16* A3 = (bf16*)(ws + (6u << 20));
  hipFuncSetAttribute((const void*)k1_kernel, hipFuncAttributeMaxDynamicSharedMemorySize, 163840);
  hipFuncSetAttribute((const void*)k2_kernel, hipFuncAttributeMaxDynamicSharedMemorySize, K2_LDS);
  hipFuncSetAttribute((const void*)k3_kernel, hipFuncAttributeMaxDynamicSharedMemorySize, K3_NST * 16384);
  k1_kernel<<<208, 512, 163840, stream>>>(x, wqkv, w1, b1, Q, K, V, A3);
  k2_kernel<<<256, 1024, K2_LDS, stream>>>(Q, K, V, A3);
  k3_kernel<<<256, 256, K3_NST * 16384, stream>>>(A3, wo, w2, b2, x, out);
}
```
